```python
import torch

def _softmax(x, axis=-1):
    return torch.softmax(x, dim=axis)
def _gelu(x):
    return torch.nn.functional.gelu(x, approximate="tanh")
def _silu(x):
    return torch.nn.functional.silu(x)
def _sigmoid(x):
    return torch.sigmoid(x)
def _layernorm(x, axis=-1, eps=1e-5):
    m = x.mean(dim=axis, keepdim=True)
    v = x.var(dim=axis, keepdim=True, unbiased=False)
    return (x - m) / torch.sqrt(v + eps)
def _scatter_add(tgt, idx, src, axis=0):
    return tgt.clone().index_add_(axis, idx, src)
def _segment_sum(src, seg, n):
    out = torch.zeros((n,) + src.shape[1:], dtype=src.dtype)
    return out.index_add_(0, seg, src)
def _pad(x, pw):
    flat = tuple(v for pair in reversed(pw) for v in pair)
    return torch.nn.functional.pad(x, flat)

kernel_name = 'parallel_sum__ssm_conv_h3__swiglu_ffn__prompt'

def setup_inputs():

    _w = (lambda a: (lambda t: t.to(torch.bfloat16) if t.is_floating_point() else t)(torch.as_tensor(a)))
    def _A_setup():
        B, S, D = 1, 2048, 512
        ks = 4
        return {
            "q": _w((lambda *s: torch.randn(*s, dtype=torch.bfloat16) * 0.05)(B, S, D)),
            "k": _w((lambda *s: torch.randn(*s, dtype=torch.bfloat16) * 0.4)(B, S, D)),
            "v": _w((lambda *s: torch.randn(*s, dtype=torch.bfloat16) * 0.4)(B, S, D)),
            "shift_k": _w((lambda *s: torch.randn(*s, dtype=torch.bfloat16) * 0.4)(D, ks) * 0.5),
            "ssm_k": _w((lambda *s: torch.randn(*s, dtype=torch.bfloat16) * 1.6)(D, S) * (1.0 / S ** 0.5)),
            "ssm_D": _w((lambda *s: torch.randn(*s, dtype=torch.bfloat16) * 0.8)(D)),
        }
    _A_in = _A_setup()

    def _B_setup():
        B, S, D, F = 1, 2048, 512, 1376
        return {
            "x": _w((lambda *s: torch.randn(*s, dtype=torch.bfloat16) * 0.05)(B, S, D)),
            "w_gate": _w((lambda *s: torch.randn(*s, dtype=torch.bfloat16) * 0.05)(D, F)),
            "w_up": _w((lambda *s: torch.randn(*s, dtype=torch.bfloat16) * 0.05)(D, F)),
            "w_down": _w((lambda *s: torch.randn(*s, dtype=torch.bfloat16) * 0.05)(F, D)),
        }
    _B_in = _B_setup()

    return {
        "A_q": _A_in["q"],
        "A_k": _A_in["k"],
        "A_v": _A_in["v"],
        "A_shift_k": _A_in["shift_k"],
        "A_ssm_k": _A_in["ssm_k"],
        "A_ssm_D": _A_in["ssm_D"],
        "B_x": _B_in["x"],
        "B_w_gate": _B_in["w_gate"],
        "B_w_up": _B_in["w_up"],
        "B_w_down": _B_in["w_down"],
    }

def reference(A_q, A_k, A_v, A_shift_k, A_ssm_k, A_ssm_D, B_x, B_w_gate, B_w_up, B_w_down):
    def _A_ref(q, k, v, shift_k, ssm_k, ssm_D):
        B, S, D = q.shape
        ks = shift_k.shape[-1]
        k_pad = _pad(k, ((0, 0), (ks - 1, 0), (0, 0)))
        k_shift = torch.zeros_like(k)
        for t in range(ks):
            k_shift = k_shift + k_pad[:, t:t + S, :] * shift_k[:, ks - 1 - t][None, None, :]
        kv = k_shift * v
        L = 2 * S
        kv_pad = _pad(kv, ((0, 0), (0, S), (0, 0)))
        kf_pad = _pad(ssm_k, ((0, 0), (0, S)))
        kv_f = (lambda x, axes=(-2,-1): torch.fft.rfft2(x.float(), dim=axes))(kv_pad[:, None, :, :], axes=(1, 2))
        kf_f = (lambda x, axes=(-2,-1): torch.fft.rfft2(x.float(), dim=axes))(kf_pad[:, None, :], axes=(1, 2))
        kf_f = (lambda x, *d: x.permute(*d))(kf_f, 1, 2, 0)
        y_f = kv_f * kf_f[None, :, :, :]
        y = (lambda x, s, axes=(-2,-1): torch.fft.irfft2(x, s=s, dim=axes).to(torch.bfloat16))(y_f, (1, L), axes=(1, 2))[:, 0, :S, :]
        y = y + kv * ssm_D[None, None, :]
        return q * y

    def _B_ref(x, w_gate, w_up, w_down):
        g = _silu(x @ w_gate)
        u = x @ w_up
        return (g * u) @ w_down

    x = A_q
    xn = _layernorm(x, axis=-1)
    return x + _A_ref(xn, A_k, A_v, A_shift_k, A_ssm_k, A_ssm_D) + _B_ref(xn, B_w_gate, B_w_up, B_w_down)
```

```cpp
#include <hip/hip_runtime.h>
#include <hip/hip_bf16.h>

typedef short v4s __attribute__((ext_vector_type(4)));
typedef short v8s __attribute__((ext_vector_type(8)));
typedef unsigned v4u __attribute__((ext_vector_type(4)));
typedef unsigned v2u __attribute__((ext_vector_type(2)));
typedef float f16v __attribute__((ext_vector_type(16)));
typedef unsigned short u16;
typedef __attribute__((address_space(3))) v4s* lds_v4s_p;

constexpr int S_ = 2048, D_ = 512, F_ = 1376;

__device__ __forceinline__ float bf2f(u16 v) { return __uint_as_float((unsigned)v << 16); }
__device__ __forceinline__ u16 f2bf(float f) { return __builtin_bit_cast(u16, (__bf16)f); }
__device__ __forceinline__ float rbf(float f) { return bf2f(f2bf(f)); }

__global__ __launch_bounds__(256) void prep_kernel(const u16* __restrict__ x, const u16* __restrict__ k,
                                                   const u16* __restrict__ v, const u16* __restrict__ shk,
                                                   u16* __restrict__ xn, u16* __restrict__ uT) {
  __shared__ u16 tile[64][72];
  const int tid = threadIdx.x, lane = tid & 63, w = tid >> 6, b = blockIdx.x;
  for (int rr = 0; rr < 2; ++rr) {
    const int row = 8 * b + 2 * w + rr;
    v8s xv = *(const v8s*)(x + row * D_ + lane * 8);
    float f[8], s = 0.f;
    for (int i = 0; i < 8; ++i) { f[i] = bf2f((u16)xv[i]); s += f[i]; }
    for (int o = 32; o; o >>= 1) s += __shfl_xor(s, o);
    const float mean = s * (1.f / D_);
    float q = 0.f;
    for (int i = 0; i < 8; ++i) { float d = f[i] - mean; q += d * d; }
    for (int o = 32; o; o >>= 1) q += __shfl_xor(q, o);
    const float mb = rbf(mean), vb = rbf(q * (1.f / D_));
    const float den = rbf(sqrtf(rbf(vb + 1e-5f)));
    v8s o8;
    for (int i = 0; i < 8; ++i) o8[i] = (short)f2bf(rbf(f[i] - mb) / den);
    *(v8s*)(xn + row * D_ + lane * 8) = o8;
  }
  const int tt = b >> 3, ct = b & 7;
  for (int p = 0; p < 2; ++p) {
    const int item = tid + 256 * p, row = item >> 3, ck = item & 7;
    const int t = 64 * tt + row, ch0 = 64 * ct + 8 * ck;
    v8s kk[4];
    for (int q = 0; q < 4; ++q) {
      const int ts = t + q - 3;
      v8s z = {0, 0, 0, 0, 0, 0, 0, 0};
      kk[q] = ts >= 0 ? *(const v8s*)(k + ts * D_ + ch0) : z;
    }
    v8s vv = *(const v8s*)(v + t * D_ + ch0);
    v8s sw[4];
    for (int q = 0; q < 4; ++q) sw[q] = *(const v8s*)(shk + ch0 * 4 + q * 8);
    for (int c = 0; c < 8; ++c) {
      float ks = 0.f;
      for (int q = 0; q < 4; ++q) {
        const int idx = c * 4 + (3 - q);
        const float wv = bf2f((u16)sw[idx >> 3][idx & 7]);
        ks = rbf(ks + rbf(bf2f((u16)kk[q][c]) * wv));
      }
      tile[8 * ck + c][row] = f2bf(ks * bf2f((u16)vv[c]));
    }
  }
  __syncthreads();
  {
    const int ch = tid >> 2, part = tid & 3;
    for (int hh = 0; hh < 2; ++hh) {
      v8s o;
      for (int i = 0; i < 8; ++i) o[i] = (short)tile[ch][part * 16 + hh * 8 + i];
      *(v8s*)(uT + (64 * ct + ch) * S_ + 64 * tt + part * 16 + hh * 8) = o;
    }
  }
}

__device__ __forceinline__ void glds16(const void* g, void* l) {
  __builtin_amdgcn_global_load_lds((const unsigned*)g, (unsigned*)l, 16, 0, 0);
}
__device__ __forceinline__ v8s trfrag(const char* base, int a0, int a1) {
  v4s a = __builtin_amdgcn_ds_read_tr16_b64_v4i16((lds_v4s_p)(base + a0));
  v4s b = __builtin_amdgcn_ds_read_tr16_b64_v4i16((lds_v4s_p)(base + a1));
  v8s r = {a[0], a[1], a[2], a[3], b[0], b[1], b[2], b[3]};
  return r;
}
#define MFMA32(a, b, c) __builtin_amdgcn_mfma_f32_32x32x16_bf16(a, b, c, 0, 0, 0)

constexpr int CS = 4160;
constexpr int CH_LDS = 8 * CS + 64 * 80;
constexpr int MAIN_LDS = 4 * CH_LDS + 16;
constexpr int NCONV = 128, G1_NT = 22, G1_MT = 16;

__global__ __launch_bounds__(256) void main_kernel(const u16* __restrict__ xn, const u16* __restrict__ uT,
                                                   const u16* __restrict__ ssk, const u16* __restrict__ ssD,
                                                   const u16* __restrict__ wg, const u16* __restrict__ wu,
                                                   u16* __restrict__ y2T, u16* __restrict__ h) {
  extern __shared__ __attribute__((aligned(16))) char sm[];
  const int tid = threadIdx.x, lane = tid & 63, w = tid >> 6;
  if (blockIdx.x < NCONV) {
    const int ch = blockIdx.x * 4 + w;
    char* cb = sm + w * CH_LDS;
    char* ub = cb + 8 * CS;
    char* zb = sm + 4 * CH_LDS;
    if (tid == 0) *(v4u*)zb = v4u{0, 0, 0, 0};
    for (int c = lane; c < 256; c += 64)
      *(v8s*)(ub + 80 * (c >> 2) + 16 * (c & 3)) = *(const v8s*)(uT + ch * S_ + 8 * c);
    for (int c = lane; c < 260; c += 64) {
      v4u hi = {0, 0, 0, 0}, lo = {0, 0, 0, 0};
      if (c < 256) hi = *(const v4u*)(ssk + ch * S_ + 2040 - 8 * c);
      if (c < 255) lo = *(const v4u*)(ssk + ch * S_ + 2032 - 8 * c);
      unsigned Y[8];
      for (int i = 0; i < 4; ++i) {
        Y[i] = __builtin_amdgcn_alignbit(hi[3 - i], hi[3 - i], 16);
        Y[4 + i] = __builtin_amdgcn_alignbit(lo[3 - i], lo[3 - i], 16);
      }
      for (int s = 0; s < 8; ++s) {
        v4u o;
        for (int i = 0; i < 4; ++i)
          o[i] = (s & 1) ? __builtin_amdgcn_alignbit(Y[(s >> 1) + i + 1], Y[(s >> 1) + i], 16) : Y[(s >> 1) + i];
        *(v4u*)(cb + s * CS + 16 * c) = o;
      }
    }
    __syncthreads();
    const int j = lane & 31, hh = lane >> 5;
    const int a = 2047 - j;
    const char* abase = cb + (a & 7) * CS + 16 * ((a >> 3) + hh);
    const char* bbase = ub + 80 * j + 16 * hh;
    f16v acc0 = {}, acc1 = {};
    for (int d = 0; d < 64; ++d) {
#pragma unroll
      for (int st = 0; st < 2; ++st) {
        const int q = 2 * d - st;
        v8s A = *(const v8s*)(abase - 32 * q);
        v8s B1 = *(const v8s*)(bbase + 80 * (32 - d) + 32 * st);
        if (j + 32 < d) B1 = *(const v8s*)zb;
        acc1 = MFMA32(A, B1, acc1);
        if (d < 32) {
          const char* p0 = (j >= d) ? (bbase - 80 * d + 32 * st) : zb;
          v8s B0 = *(const v8s*)p0;
          acc0 = MFMA32(A, B0, acc0);
        }
      }
    }
    const float Dv = bf2f(ssD[ch]);
    for (int tl = 0; tl < 2; ++tl)
      for (int g = 0; g < 4; ++g) {
        const int blk = 32 * tl + j, off = 8 * g + 4 * hh;
        v4s uu = *(const v4s*)(ub + 80 * blk + 2 * off);
        v4s o;
        for (int e = 0; e < 4; ++e) {
          const float yv = tl ? acc1[4 * g + e] : acc0[4 * g + e];
          o[e] = (short)f2bf(rbf(yv) + rbf(bf2f((u16)uu[e]) * Dv));
        }
        *(v4s*)(y2T + ch * S_ + 32 * blk + off) = o;
      }
    return;
  }
  const int bid = blockIdx.x - NCONV, mt = bid % G1_MT, nt = bid / G1_MT;
  const int row0 = mt * 128, col0 = nt * 64;
  const int wr = w >> 1, wc = w & 1;
  auto stage = [&](int kt, int buf) {
    char* base = sm + buf * 32768;
    for (int i = 0; i < 4; ++i) {
      const int p = tid + 256 * i, r = p >> 3, c = (p & 7) ^ (r & 7);
      glds16(xn + (row0 + r) * D_ + kt * 64 + c * 8, base + (w * 64 + 256 * i) * 16);
    }
    for (int i = 0; i < 2; ++i) {
      const int p = tid + 256 * i, r = p >> 3, c = (p & 7) ^ (r & 7);
      const int cc = min(col0 + c * 8, F_ - 8);
      glds16(wg + (kt * 64 + r) * F_ + cc, base + 16384 + (w * 64 + 256 * i) * 16);
      glds16(wu + (kt * 64 + r) * F_ + cc, base + 24576 + (w * 64 + 256 * i) * 16);
    }
  };
  f16v acc[2][2] = {};
  stage(0, 0);
  const int r = lane & 31, hh = lane >> 5, blk = (lane >> 4) & 1, q4 = (lane & 15) >> 2, p4 = lane & 3;
  for (int kt = 0; kt < 8; ++kt) {
    asm volatile("s_waitcnt vmcnt(0)" ::: "memory");
    __syncthreads();
    if (kt + 1 < 8) stage(kt + 1, (kt + 1) & 1);
    const char* base = sm + (kt & 1) * 32768;
#pragma unroll
    for (int ks = 0; ks < 4; ++ks) {
      v8s A[2], B[2];
      for (int m = 0; m < 2; ++m) {
        const int rr = wr * 64 + m * 32 + r;
        A[m] = *(const v8s*)(base + rr * 128 + 16 * ((2 * ks + hh) ^ (rr & 7)));
      }
      for (int n = 0; n < 2; ++n) {
        const int r0 = 16 * ks + 8 * hh + q4, r1 = r0 + 4, c = 4 * wc + 2 * blk + (p4 >> 1);
        B[n] = trfrag(base + 16384 + n * 8192, r0 * 128 + 16 * (c ^ (r0 & 7)) + 8 * (p4 & 1),
                      r1 * 128 + 16 * (c ^ (r1 & 7)) + 8 * (p4 & 1));
      }
      for (int m = 0; m < 2; ++m)
        for (int n = 0; n < 2; ++n) acc[m][n] = MFMA32(A[m], B[n], acc[m][n]);
    }
  }
  const int col = col0 + wc * 32 + r;
  if (col < F_)
    for (int m = 0; m < 2; ++m)
      for (int e = 0; e < 16; ++e) {
        const int row = row0 + wr * 64 + m * 32 + (e & 3) + 8 * (e >> 2) + 4 * hh;
        const float g = rbf(acc[m][0][e]);
        const float sg = rbf(g / (1.f + __expf(-g)));
        h[row * F_ + col] = f2bf(sg * rbf(acc[m][1][e]));
      }
}

__global__ __launch_bounds__(256) void final_kernel(const u16* __restrict__ h, const u16* __restrict__ wd,
                                                    const u16* __restrict__ x, const u16* __restrict__ xn,
                                                    const u16* __restrict__ y2T, u16* __restrict__ out) {
  __shared__ __attribute__((aligned(16))) char sm[2 * 8192];
  const int tid = threadIdx.x, lane = tid & 63, w = tid >> 6;
  const int row0 = (blockIdx.x >> 3) * 64, col0 = (blockIdx.x & 7) * 64;
  const int wr = w >> 1, wc = w & 1;
  auto stage = [&](int kt, int buf) {
    char* base = sm + buf * 8192;
    { const int r = tid >> 2, c = (tid & 3) ^ ((r >> 1) & 3);
      glds16(h + (row0 + r) * F_ + kt * 32 + c * 8, base + w * 1024); }
    { const int r = tid >> 3, c = (tid & 7) ^ (r & 7);
      glds16(wd + (kt * 32 + r) * D_ + col0 + c * 8, base + 4096 + w * 1024); }
  };
  f16v acc = {};
  stage(0, 0);
  const int r = lane & 31, hh = lane >> 5, blk = (lane >> 4) & 1, q4 = (lane & 15) >> 2, p4 = lane & 3;
  for (int kt = 0; kt < 43; ++kt) {
    asm volatile("s_waitcnt vmcnt(0)" ::: "memory");
    __syncthreads();
    if (kt + 1 < 43) stage(kt + 1, (kt + 1) & 1);
    const char* base = sm + (kt & 1) * 8192;
#pragma unroll
    for (int ks = 0; ks < 2; ++ks) {
      const int rr = wr * 32 + r;
      v8s A = *(const v8s*)(base + rr * 64 + 16 * ((2 * ks + hh) ^ ((rr >> 1) & 3)));
      const int r0 = 16 * ks + 8 * hh + q4, r1 = r0 + 4, c = 4 * wc + 2 * blk + (p4 >> 1);
      v8s B = trfrag(base + 4096, r0 * 128 + 16 * (c ^ (r0 & 7)) + 8 * (p4 & 1),
                     r1 * 128 + 16 * (c ^ (r1 & 7)) + 8 * (p4 & 1));
      acc = MFMA32(A, B, acc);
    }
  }
  const int col = col0 + wc * 32 + r;
  for (int g = 0; g < 4; ++g) {
    const int rb = row0 + wr * 32 + 8 * g + 4 * hh;
    v4s yy = *(const v4s*)(y2T + col * S_ + rb);
    for (int e = 0; e < 4; ++e) {
      const int idx = (rb + e) * D_ + col;
      const float a = rbf(bf2f(xn[idx]) * bf2f((u16)yy[e]));
      out[idx] = f2bf(rbf(bf2f(x[idx]) + a) + rbf(acc[4 * g + e]));
    }
  }
}

extern "C" void kernel_launch(void* const* d_in, const int* in_sizes, int n_in, void* d_out, int out_size,
                              void* d_ws, size_t ws_size, hipStream_t stream) {
  const u16* q = (const u16*)d_in[0];
  const u16* k = (const u16*)d_in[1];
  const u16* v = (const u16*)d_in[2];
  const u16* shk = (const u16*)d_in[3];
  const u16* ssk = (const u16*)d_in[4];
  const u16* ssD = (const u16*)d_in[5];
  const u16* wg = (const u16*)d_in[7];
  const u16* wu = (const u16*)d_in[8];
  const u16* wd = (const u16*)d_in[9];
  char* ws = (char*)d_ws;
  u16* xn = (u16*)ws;
  u16* uT = (u16*)(ws + (2 << 20));
  u16* y2T = (u16*)(ws + (4 << 20));
  u16* h = (u16*)(ws + (6 << 20));
  hipFuncSetAttribute((const void*)main_kernel, hipFuncAttributeMaxDynamicSharedMemorySize, MAIN_LDS);
  prep_kernel<<<256, 256, 0, stream>>>(q, k, v, shk, xn, uT);
  main_kernel<<<NCONV + G1_MT * G1_NT, 256, MAIN_LDS, stream>>>(xn, uT, ssk, ssD, wg, wu, y2T, h);
  final_kernel<<<256, 256, 0, stream>>>(h, wd, q, xn, y2T, (u16*)d_out);
}
```

```cpp
#include <hip/hip_runtime.h>
#include <hip/hip_bf16.h>

typedef short v4s __attribute__((ext_vector_type(4)));
typedef short v8s __attribute__((ext_vector_type(8)));
typedef unsigned v4u __attribute__((ext_vector_type(4)));
typedef unsigned v2u __attribute__((ext_vector_type(2)));
typedef float f16v __attribute__((ext_vector_type(16)));
typedef unsigned short u16;
typedef __attribute__((address_space(3))) v4s* lds_v4s_p;

constexpr int S_ = 2048, D_ = 512, F_ = 1376;

__device__ __forceinline__ float bf2f(u16 v) { return __uint_as_float((unsigned)v << 16); }
__device__ __forceinline__ u16 f2bf(float f) { return __builtin_bit_cast(u16, (__bf16)f); }
__device__ __forceinline__ float rbf(float f) { return bf2f(f2bf(f)); }

__global__ __launch_bounds__(256) void prep_kernel(const u16* __restrict__ x, const u16* __restrict__ k,
                                                   const u16* __restrict__ v, const u16* __restrict__ shk,
                                                   u16* __restrict__ xn, u16* __restrict__ uT) {
  __shared__ u16 tile[64][72];
  const int tid = threadIdx.x, lane = tid & 63, w = tid >> 6, b = blockIdx.x;
  if (b >= 256) {
    const int row = 4 * (b - 256) + w;
    v8s xv = *(const v8s*)(x + row * D_ + lane * 8);
    float f[8], s = 0.f;
    for (int i = 0; i < 8; ++i) { f[i] = bf2f((u16)xv[i]); s += f[i]; }
    for (int o = 32; o; o >>= 1) s += __shfl_xor(s, o);
    const float mean = s * (1.f / D_);
    float q = 0.f;
    for (int i = 0; i < 8; ++i) { float d = f[i] - mean; q += d * d; }
    for (int o = 32; o; o >>= 1) q += __shfl_xor(q, o);
    const float mb = rbf(mean), vb = rbf(q * (1.f / D_));
    const float den = rbf(sqrtf(rbf(vb + 1e-5f)));
    v8s o8;
    for (int i = 0; i < 8; ++i) o8[i] = (short)f2bf(rbf(f[i] - mb) / den);
    *(v8s*)(xn + row * D_ + lane * 8) = o8;
    return;
  }
  const int tt = b >> 3, ct = b & 7;
  for (int p = 0; p < 2; ++p) {
    const int item = tid + 256 * p, row = item >> 3, ck = item & 7;
    const int t = 64 * tt + row, ch0 = 64 * ct + 8 * ck;
    v8s kk[4];
    for (int q = 0; q < 4; ++q) {
      const int ts = t + q - 3;
      v8s z = {0, 0, 0, 0, 0, 0, 0, 0};
      kk[q] = ts >= 0 ? *(const v8s*)(k + ts * D_ + ch0) : z;
    }
    v8s vv = *(const v8s*)(v + t * D_ + ch0);
    v8s sw[4];
    for (int q = 0; q < 4; ++q) sw[q] = *(const v8s*)(shk + ch0 * 4 + q * 8);
    for (int c = 0; c < 8; ++c) {
      float ks = 0.f;
      for (int q = 0; q < 4; ++q) {
        const int idx = c * 4 + (3 - q);
        const float wv = bf2f((u16)sw[idx >> 3][idx & 7]);
        ks = rbf(ks + rbf(bf2f((u16)kk[q][c]) * wv));
      }
      tile[8 * ck + c][row] = f2bf(ks * bf2f((u16)vv[c]));
    }
  }
  __syncthreads();
  {
    const int ch = tid >> 2, part = tid & 3;
    for (int hh = 0; hh < 2; ++hh) {
      v8s o;
      for (int i = 0; i < 8; ++i) o[i] = (short)tile[ch][part * 16 + hh * 8 + i];
      *(v8s*)(uT + (64 * ct + ch) * S_ + 64 * tt + part * 16 + hh * 8) = o;
    }
  }
}

__device__ __forceinline__ void glds16(const void* g, void* l) {
  unsigned keep;
  const unsigned dst = __builtin_amdgcn_readfirstlane((unsigned)(size_t)l);
  asm volatile("s_mov_b32 %0, m0\n\ts_mov_b32 m0, %2\n\ts_nop 0\n\tglobal_load_lds_dwordx4 %1, off\n\ts_mov_b32 m0, %0"
               : "=&s"(keep) : "v"(g), "s"(dst) : "memory");
}
__device__ __forceinline__ v8s trfrag(const char* base, int a0, int a1) {
  v4s a = __builtin_amdgcn_ds_read_tr16_b64_v4i16((lds_v4s_p)(base + a0));
  v4s b = __builtin_amdgcn_ds_read_tr16_b64_v4i16((lds_v4s_p)(base + a1));
  v8s r = {a[0], a[1], a[2], a[3], b[0], b[1], b[2], b[3]};
  return r;
}
#define MFMA32(a, b, c) __builtin_amdgcn_mfma_f32_32x32x16_bf16(a, b, c, 0, 0, 0)

constexpr int CS = 4160;
constexpr int CH_LDS = 8 * CS + 64 * 80;
constexpr int MAIN_LDS = 4 * CH_LDS + 16;
constexpr int NCONV = 128, G1_NT = 22, G1_MT = 16;

__global__ __launch_bounds__(512) void main_kernel(const u16* __restrict__ xn, const u16* __restrict__ uT,
                                                   const u16* __restrict__ ssk, const u16* __restrict__ ssD,
                                                   const u16* __restrict__ wg, const u16* __restrict__ wu,
                                                   u16* __restrict__ y2T, u16* __restrict__ h) {
  extern __shared__ __attribute__((aligned(16))) char sm[];
  const int tid = threadIdx.x, lane = tid & 63, w = tid >> 6;
  if (blockIdx.x < NCONV) {
    const int ch = blockIdx.x * 4 + (w >> 1), hf = w & 1;
    char* cb = sm + (w >> 1) * CH_LDS;
    char* ub = cb + 8 * CS;
    char* zb = sm + 4 * CH_LDS;
    if (tid == 0) *(v4u*)zb = v4u{0, 0, 0, 0};
    for (int c = lane + 64 * hf; c < 256; c += 128)
      *(v8s*)(ub + 80 * (c >> 2) + 16 * (c & 3)) = *(const v8s*)(uT + ch * S_ + 8 * c);
    for (int c = lane + 64 * hf; c < 260; c += 128) {
      v4u hi = {0, 0, 0, 0}, lo = {0, 0, 0, 0};
      if (c < 256) hi = *(const v4u*)(ssk + ch * S_ + 2040 - 8 * c);
      if (c < 255) lo = *(const v4u*)(ssk + ch * S_ + 2032 - 8 * c);
      unsigned Y[8];
      for (int i = 0; i < 4; ++i) {
        Y[i] = __builtin_amdgcn_alignbit(hi[3 - i], hi[3 - i], 16);
        Y[4 + i] = __builtin_amdgcn_alignbit(lo[3 - i], lo[3 - i], 16);
      }
      for (int s = 0; s < 8; ++s) {
        v4u o;
        for (int i = 0; i < 4; ++i)
          o[i] = (s & 1) ? __builtin_amdgcn_alignbit(Y[(s >> 1) + i + 1], Y[(s >> 1) + i], 16) : Y[(s >> 1) + i];
        *(v4u*)(cb + s * CS + 16 * c) = o;
      }
    }
    __syncthreads();
    const int j = lane & 31, hh = lane >> 5;
    const int a = 2047 - j;
    const char* abase = cb + (a & 7) * CS + 16 * ((a >> 3) + hh);
    const char* bbase = ub + 80 * j + 16 * hh;
    f16v acc0 = {}, acc1 = {};
    const int d1lo = hf ? 0 : 48, d1hi = hf ? 48 : 64;
#pragma unroll 4
    for (int d = d1lo; d < d1hi; ++d) {
#pragma unroll
      for (int st = 0; st < 2; ++st) {
        v8s A = *(const v8s*)(abase - 32 * (2 * d - st));
        const char* p1 = (j + 32 >= d) ? (bbase + 80 * (32 - d) + 32 * st) : zb;
        v8s B1 = *(const v8s*)p1;
        acc1 = MFMA32(A, B1, acc1);
      }
    }
    if (!hf) {
#pragma unroll 4
      for (int d = 0; d < 32; ++d) {
#pragma unroll
        for (int st = 0; st < 2; ++st) {
          v8s A = *(const v8s*)(abase - 32 * (2 * d - st));
          const char* p0 = (j >= d) ? (bbase - 80 * d + 32 * st) : zb;
          v8s B0 = *(const v8s*)p0;
          acc0 = MFMA32(A, B0, acc0);
        }
      }
    }
    __syncthreads();
    if (hf) for (int e = 0; e < 16; ++e) ((float*)cb)[e * 64 + lane] = acc1[e];
    __syncthreads();
    if (hf) return;
    for (int e = 0; e < 16; ++e) acc1[e] += ((float*)cb)[e * 64 + lane];
    const float Dv = bf2f(ssD[ch]);
    for (int tl = 0; tl < 2; ++tl)
      for (int g = 0; g < 4; ++g) {
        const int blk = 32 * tl + j, off = 8 * g + 4 * hh;
        v4s uu = *(const v4s*)(ub + 80 * blk + 2 * off);
        v4s o;
        for (int e = 0; e < 4; ++e) {
          const float yv = tl ? acc1[4 * g + e] : acc0[4 * g + e];
          o[e] = (short)f2bf(rbf(yv) + rbf(bf2f((u16)uu[e]) * Dv));
        }
        *(v4s*)(y2T + ch * S_ + 32 * blk + off) = o;
      }
    return;
  }
  const int bid = blockIdx.x - NCONV, mt = bid % G1_MT, nt = bid / G1_MT;
  const int row0 = mt * 128, col0 = nt * 64;
  const int wr = w >> 1, wc = w & 1;
  auto stage = [&](int kt, int buf) {
    char* base = sm + buf * 32768;
    for (int i = 0; i < 2; ++i) {
      const int p = tid + 512 * i, r = p >> 3, c = (p & 7) ^ (r & 7);
      glds16(xn + (row0 + r) * D_ + kt * 64 + c * 8, base + (w * 64 + 512 * i) * 16);
    }
    { const int i = 0;
      const int p = tid, r = p >> 3, c = (p & 7) ^ (r & 7);
      const int cc = min(col0 + c * 8, F_ - 8);
      glds16(wg + (kt * 64 + r) * F_ + cc, base + 16384 + (w * 64 + 256 * i) * 16);
      glds16(wu + (kt * 64 + r) * F_ + cc, base + 24576 + (w * 64 + 256 * i) * 16);
    }
  };
  f16v acc[2][2] = {};
  stage(0, 0); stage(1, 1); stage(2, 2);
  const int r = lane & 31, hh = lane >> 5, blk = (lane >> 4) & 1, q4 = (lane & 15) >> 2, p4 = lane & 3;
  for (int kt = 0; kt < 8; ++kt) {
    if (kt < 6) asm volatile("s_waitcnt vmcnt(8)" ::: "memory");
    else if (kt == 6) asm volatile("s_waitcnt vmcnt(4)" ::: "memory");
    else asm volatile("s_waitcnt vmcnt(0)" ::: "memory");
    __builtin_amdgcn_s_barrier();
    if (kt + 3 < 8) stage(kt + 3, (kt + 3) & 3);
    const char* base = sm + (kt & 3) * 32768;
#pragma unroll
    for (int ks = 0; ks < 4; ++ks) {
      v8s A[2], B[2];
      for (int m = 0; m < 1; ++m) {
        const int rr = wr * 32 + r;
        A[m] = *(const v8s*)(base + rr * 128 + 16 * ((2 * ks + hh) ^ (rr & 7)));
      }
      for (int n = 0; n < 2; ++n) {
        const int r0 = 16 * ks + 8 * hh + q4, r1 = r0 + 4, c = 4 * wc + 2 * blk + (p4 >> 1);
        B[n] = trfrag(base + 16384 + n * 8192, r0 * 128 + 16 * (c ^ (r0 & 7)) + 8 * (p4 & 1),
                      r1 * 128 + 16 * (c ^ (r1 & 7)) + 8 * (p4 & 1));
      }
      for (int m = 0; m < 1; ++m)
        for (int n = 0; n < 2; ++n) acc[m][n] = MFMA32(A[m], B[n], acc[m][n]);
    }
  }
  const int col = col0 + wc * 32 + r;
  if (col < F_)
    for (int m = 0; m < 1; ++m)
      for (int e = 0; e < 16; ++e) {
        const int row = row0 + wr * 32 + (e & 3) + 8 * (e >> 2) + 4 * hh;
        const float g = rbf(acc[m][0][e]);
        const float sg = rbf(g / (1.f + __expf(-g)));
        h[row * F_ + col] = f2bf(sg * rbf(acc[m][1][e]));
      }
}

__global__ __launch_bounds__(512) void final_kernel(const u16* __restrict__ h, const u16* __restrict__ wd,
                                                    const u16* __restrict__ x, const u16* __restrict__ xn,
                                                    const u16* __restrict__ y2T, u16* __restrict__ out) {
  __shared__ __attribute__((aligned(16))) char sm[8 * 16384];
  const int tid = threadIdx.x, lane = tid & 63, w = tid >> 6;
  const int row0 = ((blockIdx.x & 7) * 4 + ((blockIdx.x >> 3) & 3)) * 64, col0 = (blockIdx.x >> 5) * 64;
  const int wr = (w >> 1) & 1, wc = w & 1, kg = w >> 2;
  auto stage = [&](int kt, int buf) {
    char* base = sm + buf * 16384;
    { const int i = 0;
      const int p = tid, r = p >> 3, c = (p & 7) ^ (r & 7);
      glds16(h + (row0 + r) * F_ + min(kt * 64 + c * 8, F_ - 8), base + (w * 64 + 256 * i) * 16);
      glds16(wd + min(kt * 64 + r, F_ - 1) * D_ + col0 + c * 8, base + 8192 + (w * 64 + 256 * i) * 16);
    }
  };
  f16v acc = {};
  for (int i = 0; i < 7; ++i) stage(i, i);
  const int r = lane & 31, hh = lane >> 5, blk = (lane >> 4) & 1, q4 = (lane & 15) >> 2, p4 = lane & 3;
  for (int kt = 0; kt < 22; ++kt) {
    if (kt < 16) asm volatile("s_waitcnt vmcnt(12)" ::: "memory");
    else asm volatile("s_waitcnt vmcnt(0)" ::: "memory");
    __builtin_amdgcn_s_barrier();
    if (kt + 7 < 22) stage(kt + 7, (kt + 7) & 7);
    const char* base = sm + (kt & 7) * 16384;
    if (kt == 21 && kg) break;
#pragma unroll
    for (int ks = 2 * kg; ks < 2 * kg + 2; ++ks) {
      const int rr = wr * 32 + r;
      v8s A = *(const v8s*)(base + rr * 128 + 16 * ((2 * ks + hh) ^ (rr & 7)));
      const int r0 = 16 * ks + 8 * hh + q4, r1 = r0 + 4, c = 4 * wc + 2 * blk + (p4 >> 1);
      v8s B = trfrag(base + 8192, r0 * 128 + 16 * (c ^ (r0 & 7)) + 8 * (p4 & 1),
                     r1 * 128 + 16 * (c ^ (r1 & 7)) + 8 * (p4 & 1));
      acc = MFMA32(A, B, acc);
    }
  }
  __syncthreads();
  if (kg) for (int e = 0; e < 16; ++e) ((float*)sm)[((w & 3) * 16 + e) * 64 + lane] = acc[e];
  __syncthreads();
  if (kg) return;
  for (int e = 0; e < 16; ++e) acc[e] += ((float*)sm)[(w * 16 + e) * 64 + lane];
  const int col = col0 + wc * 32 + r;
  for (int g = 0; g < 4; ++g) {
    const int rb = row0 + wr * 32 + 8 * g + 4 * hh;
    v4s yy = *(const v4s*)(y2T + col * S_ + rb);
    for (int e = 0; e < 4; ++e) {
      const int idx = (rb + e) * D_ + col;
      const float a = rbf(bf2f(xn[idx]) * bf2f((u16)yy[e]));
      out[idx] = f2bf(rbf(bf2f(x[idx]) + a) + rbf(acc[4 * g + e]));
    }
  }
}

extern "C" void kernel_launch(void* const* d_in, const int* in_sizes, int n_in, void* d_out, int out_size,
                              void* d_ws, size_t ws_size, hipStream_t stream) {
  const u16* q = (const u16*)d_in[0];
  const u16* k = (const u16*)d_in[1];
  const u16* v = (const u16*)d_in[2];
  const u16* shk = (const u16*)d_in[3];
  const u16* ssk = (const u16*)d_in[4];
  const u16* ssD = (const u16*)d_in[5];
  const u16* wg = (const u16*)d_in[7];
  const u16* wu = (const u16*)d_in[8];
  const u16* wd = (const u16*)d_in[9];
  char* ws = (char*)d_ws;
  u16* xn = (u16*)ws;
  u16* uT = (u16*)(ws + (2 << 20));
  u16* y2T = (u16*)(ws + (4 << 20));
  u16* h = (u16*)(ws + (6 << 20));
  hipFuncSetAttribute((const void*)main_kernel, hipFuncAttributeMaxDynamicSharedMemorySize, MAIN_LDS);
  prep_kernel<<<768, 256, 0, stream>>>(q, k, v, shk, xn, uT);
  main_kernel<<<NCONV + G1_MT * G1_NT, 512, MAIN_LDS, stream>>>(xn, uT, ssk, ssD, wg, wu, y2T, h);
  final_kernel<<<256, 512, 0, stream>>>(h, wd, q, xn, y2T, (u16*)d_out);
}
```
